```python
import torch

def _softmax(x, axis=-1):
    return torch.softmax(x, dim=axis)
def _gelu(x):
    return torch.nn.functional.gelu(x, approximate="tanh")
def _silu(x):
    return torch.nn.functional.silu(x)
def _sigmoid(x):
    return torch.sigmoid(x)
def _layernorm(x, axis=-1, eps=1e-5):
    m = x.mean(dim=axis, keepdim=True)
    v = x.var(dim=axis, keepdim=True, unbiased=False)
    return (x - m) / torch.sqrt(v + eps)
def _scatter_add(tgt, idx, src, axis=0):
    return tgt.clone().index_add_(axis, idx, src)
def _segment_sum(src, seg, n):
    out = torch.zeros((n,) + src.shape[1:], dtype=src.dtype)
    return out.index_add_(0, seg, src)
def _pad(x, pw):
    flat = tuple(v for pair in reversed(pw) for v in pair)
    return torch.nn.functional.pad(x, flat)

kernel_name = 'standalone__channel_attn_se__vision'

def setup_inputs():

    _w = (lambda a: (lambda t: t.to(torch.bfloat16) if t.is_floating_point() else t)(torch.as_tensor(a)))
    H = W = 256
    C = 384
    r = max(C // 16, 4)
    return {
        "x": _w((lambda *s: torch.randn(*s, dtype=torch.bfloat16) * 1.6)(1, H, W, C)),
        "w_down": _w((lambda *s: torch.randn(*s, dtype=torch.bfloat16) * 3.2)(C, r)),
        "w_up": _w((lambda *s: torch.randn(*s, dtype=torch.bfloat16) * 1.6)(r, C)),
    }

def reference(x, w_down, w_up):
    s = x.mean(axis=(1, 2))
    h = _silu(torch.einsum('bc,cr->br', s, w_down))
    g = _sigmoid(torch.einsum('br,rc->bc', h, w_up))
    return x * g[:, None, None, :]
```

```cpp
#include <hip/hip_runtime.h>
#include <hip/hip_bf16.h>
#include <stdint.h>


#define C_CH 384
#define R_CH 24
#define NPIX 65536

typedef __attribute__((ext_vector_type(8))) unsigned short u16x8;
typedef __attribute__((ext_vector_type(4))) unsigned int u32x4;

__device__ __forceinline__ float bf2f(unsigned short u) { return __uint_as_float(((unsigned)u) << 16); }
__device__ __forceinline__ unsigned short f2bf(float f) { return __bfloat16_as_ushort(__float2bfloat16(f)); }
__device__ __forceinline__ float rbf(float f) { return bf2f(f2bf(f)); }

__global__ __launch_bounds__(384) void k_reduce(const unsigned short* __restrict__ x,
                                                const unsigned short* __restrict__ wd,
                                                const unsigned short* __restrict__ wu,
                                                float* __restrict__ sums, unsigned* __restrict__ counter,
                                                float* __restrict__ gate, int pix_per_block) {
    const int t = threadIdx.x;
    const int cg = t % 48;
    const int pr = t / 48;
    float acc[8];
#pragma unroll
    for (int j = 0; j < 8; ++j) acc[j] = 0.f;
    const int p0 = blockIdx.x * pix_per_block;
    for (int p = pr; p < pix_per_block; p += 8) {
        const u16x8 v = *reinterpret_cast<const u16x8*>(x + (size_t)(p0 + p) * C_CH + cg * 8);
#pragma unroll
        for (int j = 0; j < 8; ++j) acc[j] += bf2f(v[j]);
    }
    __shared__ float red[8][C_CH];
#pragma unroll
    for (int j = 0; j < 8; ++j) red[pr][cg * 8 + j] = acc[j];
    __syncthreads();
    if (t < C_CH) {
        float s = 0.f;
#pragma unroll
        for (int k = 0; k < 8; ++k) s += red[k][t];
        atomicAdd(&sums[t], s);
    }
    __threadfence();
    __syncthreads();
    __shared__ unsigned last;
    if (t == 0) { unsigned old = atomicAdd(counter, 1u); last = (old == gridDim.x - 1) ? 1u : 0u; }
    __syncthreads();
    if (last) {
        __threadfence();
        __shared__ float sm[C_CH];
        __shared__ float hh[R_CH];
        if (t < C_CH) {
            float s = __hip_atomic_load(&sums[t], __ATOMIC_RELAXED, __HIP_MEMORY_SCOPE_AGENT);
            sm[t] = rbf(s * (1.0f / NPIX));
        }
        __syncthreads();
        if (t < R_CH) {
            float a = 0.f;
            for (int c = 0; c < C_CH; ++c) a += sm[c] * bf2f(wd[c * R_CH + t]);
            a = rbf(a);
            float h = a / (1.f + __expf(-a));
            hh[t] = rbf(h);
        }
        __syncthreads();
        if (t < C_CH) {
            float a = 0.f;
            for (int r = 0; r < R_CH; ++r) a += hh[r] * bf2f(wu[r * C_CH + t]);
            a = rbf(a);
            float g = 1.f / (1.f + __expf(-a));
            gate[t] = rbf(g);
        }
    }
}

__global__ __launch_bounds__(384) void k_scale(const unsigned short* __restrict__ x, const float* __restrict__ gate,
                                               unsigned short* __restrict__ out, int pix_per_block) {
    const int t = threadIdx.x;
    const int cg = t % 48;
    const int pr = t / 48;
    float g[8];
#pragma unroll
    for (int j = 0; j < 8; ++j) g[j] = gate[cg * 8 + j];
    const int p0 = blockIdx.x * pix_per_block;
    for (int p = pr; p < pix_per_block; p += 8) {
        const size_t off = (size_t)(p0 + p) * C_CH + cg * 8;
        const u16x8 v = *reinterpret_cast<const u16x8*>(x + off);
        u16x8 o;
#pragma unroll
        for (int j = 0; j < 8; ++j) o[j] = f2bf(bf2f(v[j]) * g[j]);
        *reinterpret_cast<u16x8*>(out + off) = o;
    }
}

extern "C" void kernel_launch(void* const* d_in, const int* in_sizes, int n_in,
                              void* d_out, int out_size, void* d_ws, size_t ws_size,
                              hipStream_t stream) {
    const unsigned short* x = (const unsigned short*)d_in[0];
    const unsigned short* wd = (const unsigned short*)d_in[1];
    const unsigned short* wu = (const unsigned short*)d_in[2];
    unsigned short* out = (unsigned short*)d_out;
    float* sums = (float*)d_ws;
    unsigned* counter = (unsigned*)d_ws + 384;
    float* gate = (float*)d_ws + 512;
    hipMemsetAsync(d_ws, 0, 2048, stream);
    k_reduce<<<1024, 384, 0, stream>>>(x, wd, wu, sums, counter, gate, NPIX / 1024);
    k_scale<<<2048, 384, 0, stream>>>(x, gate, out, NPIX / 2048);
}
```

```cpp
#include <hip/hip_runtime.h>
#include <hip/hip_bf16.h>
#include <stdint.h>


#define C_CH 384
#define R_CH 24
#define NPIX 65536
#define NBLK 256
#define NTHR 768
#define PPB (NPIX / NBLK)
#define NREP 8

typedef __attribute__((ext_vector_type(8))) unsigned short u16x8;
typedef __attribute__((ext_vector_type(4))) unsigned int u32x4;

__device__ __forceinline__ float bf2f(unsigned short u) { return __uint_as_float(((unsigned)u) << 16); }
__device__ __forceinline__ unsigned short f2bf(float f) { return __bfloat16_as_ushort(__float2bfloat16(f)); }
__device__ __forceinline__ float rbf(float f) { return bf2f(f2bf(f)); }

__global__ __launch_bounds__(NTHR) void k_fused(const unsigned short* __restrict__ x,
                                                const unsigned short* __restrict__ wd,
                                                const unsigned short* __restrict__ wu,
                                                unsigned short* __restrict__ out,
                                                float* __restrict__ sums, unsigned* __restrict__ cnt) {
    const int t = threadIdx.x;
    const int cg = t % 48;
    const int pr = t / 48;
    const int p0 = blockIdx.x * PPB;
    u32x4 v[16];
#pragma unroll
    for (int k = 0; k < 16; ++k)
        v[k] = __builtin_nontemporal_load(reinterpret_cast<const u32x4*>(x + (size_t)(p0 + pr + 16 * k) * C_CH + cg * 8));

    __shared__ float red[16][C_CH];
    __shared__ unsigned short swd[C_CH * R_CH];
    __shared__ unsigned short swu[C_CH * R_CH];
    __shared__ float sm[C_CH];
    __shared__ float hp[32][R_CH];
    __shared__ float hh[R_CH];
    __shared__ float gg[C_CH];

    for (int i = t; i < C_CH * R_CH / 8; i += NTHR) {
        *reinterpret_cast<u16x8*>(&swd[i * 8]) = *reinterpret_cast<const u16x8*>(wd + i * 8);
        *reinterpret_cast<u16x8*>(&swu[i * 8]) = *reinterpret_cast<const u16x8*>(wu + i * 8);
    }

    float acc[8];
#pragma unroll
    for (int j = 0; j < 8; ++j) acc[j] = 0.f;
#pragma unroll
    for (int k = 0; k < 16; ++k)
#pragma unroll
        for (int j = 0; j < 4; ++j) {
            acc[2 * j]     += __uint_as_float(v[k][j] << 16);
            acc[2 * j + 1] += __uint_as_float(v[k][j] & 0xffff0000u);
        }
#pragma unroll
    for (int k = 0; k < 16; ++k) asm volatile("" : "+v"(v[k]));
#pragma unroll
    for (int j = 0; j < 8; ++j) red[pr][cg * 8 + j] = acc[j];
    __syncthreads();
    const int rep = blockIdx.x & (NREP - 1);
    if (t < C_CH) {
        float s = 0.f;
#pragma unroll
        for (int k = 0; k < 16; ++k) s += red[k][t];
        atomicAdd(&sums[rep * C_CH + t], s);
    }
    asm volatile("s_waitcnt vmcnt(0)" ::: "memory");
    __syncthreads();
    if (t == 0) __hip_atomic_fetch_add(&cnt[rep * 64], 1u, __ATOMIC_RELAXED, __HIP_MEMORY_SCOPE_AGENT);
    if (t < 64) {
        unsigned spins = 0;
        for (;;) {
            unsigned c = (t < NREP) ? __hip_atomic_load(&cnt[t * 64], __ATOMIC_RELAXED, __HIP_MEMORY_SCOPE_AGENT) : (NBLK / NREP);
            if (__all(c >= (unsigned)(NBLK / NREP))) break;
            if (++spins > (1u << 20)) break;
            __builtin_amdgcn_s_sleep(1);
        }
    }
    __syncthreads();
    if (t < C_CH) {
        float s = 0.f;
#pragma unroll
        for (int r = 0; r < NREP; ++r)
            s += __hip_atomic_load(&sums[r * C_CH + t], __ATOMIC_RELAXED, __HIP_MEMORY_SCOPE_AGENT);
        sm[t] = rbf(s * (1.0f / NPIX));
    }
    __syncthreads();
    {
        const int r = t % R_CH, part = t / R_CH;
        float a = 0.f;
#pragma unroll
        for (int i = 0; i < 12; ++i) {
            const int c = part * 12 + i;
            a += sm[c] * bf2f(swd[c * R_CH + r]);
        }
        hp[part][r] = a;
    }
    __syncthreads();
    if (t < R_CH) {
        float a = 0.f;
#pragma unroll
        for (int k = 0; k < 32; ++k) a += hp[k][t];
        a = rbf(a);
        hh[t] = rbf(a / (1.f + __expf(-a)));
    }
    __syncthreads();
    if (t < C_CH) {
        float a = 0.f;
#pragma unroll
        for (int r = 0; r < R_CH; ++r) a += hh[r] * bf2f(swu[r * C_CH + t]);
        a = rbf(a);
        gg[t] = rbf(1.f / (1.f + __expf(-a)));
    }
    __syncthreads();
    float g[8];
#pragma unroll
    for (int j = 0; j < 8; ++j) g[j] = gg[cg * 8 + j];
#pragma unroll
    for (int k = 0; k < 16; ++k) {
        u32x4 o;
#pragma unroll
        for (int j = 0; j < 4; ++j) {
            const unsigned lo = f2bf(__uint_as_float(v[k][j] << 16) * g[2 * j]);
            const unsigned hi = f2bf(__uint_as_float(v[k][j] & 0xffff0000u) * g[2 * j + 1]);
            o[j] = lo | (hi << 16);
        }
        *reinterpret_cast<u32x4*>(out + (size_t)(p0 + pr + 16 * k) * C_CH + cg * 8) = o;
    }
}

extern "C" void kernel_launch(void* const* d_in, const int* in_sizes, int n_in,
                              void* d_out, int out_size, void* d_ws, size_t ws_size,
                              hipStream_t stream) {
    const unsigned short* x = (const unsigned short*)d_in[0];
    const unsigned short* wd = (const unsigned short*)d_in[1];
    const unsigned short* wu = (const unsigned short*)d_in[2];
    unsigned short* out = (unsigned short*)d_out;
    float* sums = (float*)d_ws;
    unsigned* cnt = (unsigned*)d_ws + NREP * C_CH;
    hipMemsetAsync(d_ws, 0, NREP * C_CH * 4 + NREP * 256, stream);
    k_fused<<<NBLK, NTHR, 0, stream>>>(x, wd, wu, out, sums, cnt);
}
```
